```python
import torch

def _softmax(x, axis=-1):
    return torch.softmax(x, dim=axis)
def _gelu(x):
    return torch.nn.functional.gelu(x, approximate="tanh")
def _silu(x):
    return torch.nn.functional.silu(x)
def _sigmoid(x):
    return torch.sigmoid(x)
def _layernorm(x, axis=-1, eps=1e-5):
    m = x.mean(dim=axis, keepdim=True)
    v = x.var(dim=axis, keepdim=True, unbiased=False)
    return (x - m) / torch.sqrt(v + eps)
def _scatter_add(tgt, idx, src, axis=0):
    return tgt.clone().index_add_(axis, idx, src)
def _segment_sum(src, seg, n):
    out = torch.zeros((n,) + src.shape[1:], dtype=src.dtype)
    return out.index_add_(0, seg, src)
def _pad(x, pw):
    flat = tuple(v for pair in reversed(pw) for v in pair)
    return torch.nn.functional.pad(x, flat)

kernel_name = 'gather_scatter__cheap_equivariant_egnn__graph'

def setup_inputs():

    _w = (lambda a: (lambda t: t.to(torch.bfloat16) if t.is_floating_point() else t)(torch.as_tensor(a)))
    N, deg, D, De = 10000, 16, 256, max(0, 1)
    E = N * deg
    return {
        "h": _w((lambda *s: torch.randn(*s, dtype=torch.bfloat16) * 0.2)(N, D)),
        "pos": _w((lambda *s: torch.randn(*s, dtype=torch.bfloat16) * 0.8)(N, 3)),
        "e_attr": _w((lambda *s: torch.randn(*s, dtype=torch.bfloat16) * 0.8)(E, De)),
        "src": _w((lambda lo, hi, *s: torch.randint(lo, hi, s))(0, N, E).to(torch.int64)),
        "dst": _w((lambda lo, hi, *s: torch.randint(lo, hi, s))(0, N, E).to(torch.int64)),
        "w_e": _w(0.1 * (lambda *s: torch.randn(*s, dtype=torch.bfloat16) * 0.8)(2 * D + 1 + De, D)),
        "w_x": _w(0.1 * (lambda *s: torch.randn(*s, dtype=torch.bfloat16) * 0.2)(D, 1)),
        "w_h": _w(0.1 * (lambda *s: torch.randn(*s, dtype=torch.bfloat16) * 0.2)(2 * D, D)),
    }

def reference(h, pos, e_attr, src, dst, w_e, w_x, w_h):
    N, D = h.shape[0], h.shape[1]
    diff = pos[dst] - pos[src]
    dist2 = torch.sum(diff * diff, axis=-1, keepdims=True)
    e_in = torch.concatenate([h[dst], h[src], dist2, e_attr], axis=-1)
    m_ij = _silu(e_in @ w_e)
    coord_scale = m_ij @ w_x
    pos_new = pos + _segment_sum(diff * coord_scale, dst, N)
    m_i = _segment_sum(m_ij, dst, N)
    h_new = h + _silu(torch.concatenate([h, m_i], axis=-1) @ w_h)
    return h_new, pos_new
```

```cpp
#include <hip/hip_runtime.h>
#include <hip/hip_bf16.h>
#include <stdint.h>


typedef unsigned short u16;
using bf16x8 = __attribute__((ext_vector_type(8))) short;
using f32x16 = __attribute__((ext_vector_type(16))) float;
using f32x4 = __attribute__((ext_vector_type(4))) float;

#define D 256
#define CAP 64

__device__ __forceinline__ float bf2f(u16 v) { return __uint_as_float(((unsigned)v) << 16); }
__device__ __forceinline__ u16 f2bf(float f) {
    unsigned u = __float_as_uint(f);
    return (u16)((u + 0x7FFFu + ((u >> 16) & 1u)) >> 16);
}
__device__ __forceinline__ float rbf(float f) { return bf2f(f2bf(f)); }
__device__ __forceinline__ float silu_f(float x) { return x / (1.0f + __expf(-x)); }

__global__ void k0_prep(const u16* __restrict__ w_e, const u16* __restrict__ w_h, int* __restrict__ cnt,
                        u16* __restrict__ wf, int n_nodes) {
    int tid = blockIdx.x * blockDim.x + threadIdx.x;
    if (tid < n_nodes + 1) cnt[tid] = 0;
    if (tid < 32 * 16 * 64) {
        int lane = tid & 63, ks = (tid >> 6) & 15, nt = tid >> 10;
        const u16* W = (nt < 16) ? (w_e + (size_t)(nt >> 3) * 256 * D) : (w_h + (size_t)((nt - 16) >> 3) * 256 * D);
        int n = (nt & 7) * 32 + (lane & 31);
        int k0 = ks * 16 + 8 * (lane >> 5);
        bf16x8 v;
#pragma unroll
        for (int j = 0; j < 8; ++j) v[j] = (short)W[(size_t)(k0 + j) * D + n];
        *(bf16x8*)(wf + (size_t)tid * 8) = v;
    }
}

__global__ __launch_bounds__(384) void k1_gemm_bucket(const u16* __restrict__ h, const u16* __restrict__ wf,
                                                      const int* __restrict__ src, const int* __restrict__ dst,
                                                      const u16* __restrict__ e_attr, int* __restrict__ cnt,
                                                      int2* __restrict__ bucket, int* __restrict__ ovf,
                                                      float* __restrict__ Pt, u16* __restrict__ Qt,
                                                      float* __restrict__ Rt, int n_nodes, int n_edges,
                                                      int gemm_blocks) {
    int lane = threadIdx.x & 63, wid = threadIdx.x >> 6;
    if ((int)blockIdx.x >= gemm_blocks) {
        int nb = gridDim.x - gemm_blocks;
        for (int e = (blockIdx.x - gemm_blocks) * blockDim.x + threadIdx.x; e < n_edges; e += nb * blockDim.x) {
            int d = dst[e];
            int r = atomicAdd(&cnt[d], 1);
            if (r < CAP) {
                bucket[(size_t)d * CAP + r] = make_int2(src[e], (int)e_attr[e]);
            } else {
                int o = atomicAdd(&cnt[n_nodes], 1);
                ovf[o] = e;
            }
        }
        return;
    }
    int node0 = blockIdx.x * 32;
    int node = min(node0 + (lane & 31), n_nodes - 1);
    f32x16 acc[4];
#pragma unroll
    for (int t = 0; t < 4; ++t)
#pragma unroll
        for (int r = 0; r < 16; ++r) acc[t][r] = 0.f;
    const u16* hrow = h + (size_t)node * D + 8 * (lane >> 5);
#pragma unroll 4
    for (int ks = 0; ks < 16; ++ks) {
        bf16x8 hb = *(const bf16x8*)(hrow + ks * 16);
#pragma unroll
        for (int t = 0; t < 4; ++t) {
            int nt = wid * 4 + t;
            bf16x8 wa = *(const bf16x8*)(wf + ((size_t)(nt * 16 + ks) * 64 + lane) * 8);
            acc[t] = __builtin_amdgcn_mfma_f32_32x32x16_bf16(wa, hb, acc[t], 0, 0, 0);
        }
    }
    int mynode = node0 + (lane & 31);
    if (mynode < n_nodes) {
#pragma unroll
        for (int t = 0; t < 4; ++t) {
            int nt = wid * 4 + t;
            int which = nt >> 3;
#pragma unroll
            for (int g = 0; g < 4; ++g) {
                int n = (nt & 7) * 32 + 8 * g + 4 * (lane >> 5);
                if (which == 1) {
                    ushort4 o;
                    o.x = f2bf(acc[t][4 * g + 0]);
                    o.y = f2bf(acc[t][4 * g + 1]);
                    o.z = f2bf(acc[t][4 * g + 2]);
                    o.w = f2bf(acc[t][4 * g + 3]);
                    *(ushort4*)(Qt + (size_t)mynode * D + n) = o;
                } else {
                    float* T = which == 0 ? Pt : Rt;
                    *(float4*)(T + (size_t)mynode * D + n) =
                        make_float4(acc[t][4 * g + 0], acc[t][4 * g + 1], acc[t][4 * g + 2], acc[t][4 * g + 3]);
                }
            }
        }
    }
}

#define T2 32
__global__ __launch_bounds__(256) void k2_edge_node(const u16* __restrict__ h, const u16* __restrict__ pos,
                                                    const int* __restrict__ src, const int* __restrict__ dst,
                                                    const u16* __restrict__ e_attr, const u16* __restrict__ w_e,
                                                    const u16* __restrict__ w_x, const u16* __restrict__ wf,
                                                    const int* __restrict__ cnt, const int2* __restrict__ bucket,
                                                    const int* __restrict__ ovf, const float* __restrict__ Pt,
                                                    const u16* __restrict__ Qt, const float* __restrict__ Rt,
                                                    u16* __restrict__ out_h, u16* __restrict__ out_pos,
                                                    int n_nodes) {
    __shared__ __attribute__((aligned(16))) u16 sm[T2][D + 8];
    int lane = threadIdx.x & 63, wid = threadIdx.x >> 6;
    int half = lane >> 5, c8 = (lane & 31) * 8;
    float wd[8], we[8], wx[8];
#pragma unroll
    for (int j = 0; j < 8; ++j) {
        wd[j] = bf2f(w_e[(size_t)512 * D + c8 + j]);
        we[j] = bf2f(w_e[(size_t)513 * D + c8 + j]);
        wx[j] = bf2f(w_x[c8 + j]);
    }
    int node0 = blockIdx.x * T2;
    for (int t = wid; t < T2; t += 4) {
        int i = node0 + t;
        float acc[8];
#pragma unroll
        for (int j = 0; j < 8; ++j) acc[j] = 0.f;
        if (i < n_nodes) {
            int c = cnt[i];
            float p0 = bf2f(pos[i * 3 + 0]), p1 = bf2f(pos[i * 3 + 1]), p2 = bf2f(pos[i * 3 + 2]);
            float P[8];
            {
                float4 a = *(const float4*)(Pt + (size_t)i * D + c8);
                float4 b = *(const float4*)(Pt + (size_t)i * D + c8 + 4);
                P[0] = a.x; P[1] = a.y; P[2] = a.z; P[3] = a.w;
                P[4] = b.x; P[5] = b.y; P[6] = b.z; P[7] = b.w;
            }
            float dp0 = 0.f, dp1 = 0.f, dp2 = 0.f;
            int cb = min(c, CAP);
            int nchunk = 1;
            int novf = 0;
            if (c > CAP) { novf = cnt[n_nodes]; nchunk += (novf + 63) / 64; }
            for (int ch = 0; ch < nchunk; ++ch) {
                int s = 0; float ea = 0.f; bool valid;
                if (ch == 0) {
                    valid = lane < cb;
                    if (valid) { int2 b = bucket[(size_t)i * CAP + lane]; s = b.x; ea = bf2f((u16)b.y); }
                } else {
                    int o = (ch - 1) * 64 + lane;
                    valid = false;
                    if (o < novf) {
                        int e = ovf[o];
                        if (dst[e] == i) { valid = true; s = src[e]; ea = bf2f(e_attr[e]); }
                    }
                }
                float d0 = 0.f, d1 = 0.f, d2 = 0.f, dist2 = 0.f;
                if (valid) {
                    d0 = rbf(p0 - bf2f(pos[s * 3 + 0]));
                    d1 = rbf(p1 - bf2f(pos[s * 3 + 1]));
                    d2 = rbf(p2 - bf2f(pos[s * 3 + 2]));
                    dist2 = rbf(rbf(rbf(d0 * d0) + rbf(d1 * d1)) + rbf(d2 * d2));
                }
                unsigned long long vmask = __ballot(valid);
                int hi = vmask ? (64 - __builtin_clzll(vmask)) : 0;
                for (int p = 0; p * 2 < hi; ++p) {
                    int ei = 2 * p + half;
                    int se = __shfl(s, ei);
                    float ds = __shfl(dist2, ei), eae = __shfl(ea, ei);
                    float e0 = __shfl(d0, ei), e1 = __shfl(d1, ei), e2 = __shfl(d2, ei);
                    bool v = (vmask >> ei) & 1ull;
                    float part = 0.f;
                    if (v) {
                        bf16x8 q = *(const bf16x8*)(Qt + (size_t)se * D + c8);
#pragma unroll
                        for (int j = 0; j < 8; ++j) {
                            float x = P[j] + bf2f((u16)q[j]) + ds * wd[j] + eae * we[j];
                            float m = silu_f(x);
                            acc[j] += m;
                            part += m * wx[j];
                        }
                    }
#pragma unroll
                    for (int o = 16; o > 0; o >>= 1) part += __shfl_xor(part, o);
                    dp0 += e0 * part; dp1 += e1 * part; dp2 += e2 * part;
                }
            }
#pragma unroll
            for (int j = 0; j < 8; ++j) acc[j] += __shfl_xor(acc[j], 32);
            dp0 += __shfl_xor(dp0, 32); dp1 += __shfl_xor(dp1, 32); dp2 += __shfl_xor(dp2, 32);
            if (lane == 0) {
                out_pos[i * 3 + 0] = f2bf(p0 + dp0);
                out_pos[i * 3 + 1] = f2bf(p1 + dp1);
                out_pos[i * 3 + 2] = f2bf(p2 + dp2);
            }
        }
        if (half == 0) {
            bf16x8 o;
#pragma unroll
            for (int j = 0; j < 8; ++j) o[j] = (short)f2bf(acc[j]);
            *(bf16x8*)(&sm[t][c8]) = o;
        }
    }
    __syncthreads();
    f32x16 acc2[2];
#pragma unroll
    for (int t = 0; t < 2; ++t)
#pragma unroll
        for (int r = 0; r < 16; ++r) acc2[t][r] = 0.f;
#pragma unroll 4
    for (int ks = 0; ks < 16; ++ks) {
        bf16x8 mb = *(const bf16x8*)(&sm[lane & 31][ks * 16 + 8 * half]);
#pragma unroll
        for (int t = 0; t < 2; ++t) {
            int nt = 24 + wid * 2 + t;
            bf16x8 wa = *(const bf16x8*)(wf + ((size_t)(nt * 16 + ks) * 64 + lane) * 8);
            acc2[t] = __builtin_amdgcn_mfma_f32_32x32x16_bf16(wa, mb, acc2[t], 0, 0, 0);
        }
    }
    int mynode = node0 + (lane & 31);
    if (mynode < n_nodes) {
#pragma unroll
        for (int t = 0; t < 2; ++t) {
#pragma unroll
            for (int g = 0; g < 4; ++g) {
                int n = (wid * 2 + t) * 32 + 8 * g + 4 * half;
                float4 r = *(const float4*)(Rt + (size_t)mynode * D + n);
                ushort4 hh = *(const ushort4*)(h + (size_t)mynode * D + n);
                ushort4 o;
                o.x = f2bf(bf2f(hh.x) + silu_f(acc2[t][4 * g + 0] + r.x));
                o.y = f2bf(bf2f(hh.y) + silu_f(acc2[t][4 * g + 1] + r.y));
                o.z = f2bf(bf2f(hh.z) + silu_f(acc2[t][4 * g + 2] + r.z));
                o.w = f2bf(bf2f(hh.w) + silu_f(acc2[t][4 * g + 3] + r.w));
                *(ushort4*)(out_h + (size_t)mynode * D + n) = o;
            }
        }
    }
}

extern "C" void kernel_launch(void* const* d_in, const int* in_sizes, int n_in, void* d_out, int out_size,
                              void* d_ws, size_t ws_size, hipStream_t stream) {
    const u16* h = (const u16*)d_in[0];
    const u16* pos = (const u16*)d_in[1];
    const u16* e_attr = (const u16*)d_in[2];
    const int* src = (const int*)d_in[3];
    const int* dst = (const int*)d_in[4];
    const u16* w_e = (const u16*)d_in[5];
    const u16* w_x = (const u16*)d_in[6];
    const u16* w_h = (const u16*)d_in[7];
    int n_nodes = in_sizes[0] / D;
    int n_edges = in_sizes[3];
    u16* out_h = (u16*)d_out;
    u16* out_pos = out_h + (size_t)n_nodes * D;

    char* ws = (char*)d_ws;
    size_t off = 0;
    auto take = [&](size_t bytes) { char* p = ws + off; off += (bytes + 255) & ~(size_t)255; return p; };
    int* cnt = (int*)take((size_t)(n_nodes + 1) * 4);
    int2* bucket = (int2*)take((size_t)n_nodes * CAP * 8);
    int* ovf = (int*)take((size_t)n_edges * 4);
    float* Pt = (float*)take((size_t)n_nodes * D * 4);
    float* Rt = (float*)take((size_t)n_nodes * D * 4);
    u16* Qt = (u16*)take((size_t)n_nodes * D * 2);
    u16* wf = (u16*)take((size_t)32 * 16 * 64 * 8 * 2);

    int k0_items = max(n_nodes + 1, 32 * 16 * 64);
    k0_prep<<<(k0_items + 255) / 256, 256, 0, stream>>>(w_e, w_h, cnt, wf, n_nodes);
    int gemm_blocks = (n_nodes + 31) / 32;
    int fill_blocks = 64;
    k1_gemm_bucket<<<gemm_blocks + fill_blocks, 384, 0, stream>>>(h, wf, src, dst, e_attr, cnt, bucket, ovf, Pt, Qt,
                                                                  Rt, n_nodes, n_edges, gemm_blocks);
    k2_edge_node<<<(n_nodes + T2 - 1) / T2, 256, 0, stream>>>(h, pos, src, dst, e_attr, w_e, w_x, wf, cnt, bucket,
                                                              ovf, Pt, Qt, Rt, out_h, out_pos, n_nodes);
}
```

```cpp
#include <hip/hip_runtime.h>
#include <hip/hip_bf16.h>
#include <stdint.h>

typedef unsigned short u16;
using bf16x8 = __attribute__((ext_vector_type(8))) short;
using f32x16 = __attribute__((ext_vector_type(16))) float;
using f32x4 = __attribute__((ext_vector_type(4))) float;
typedef float f2 __attribute__((ext_vector_type(2)));

#define D 256
#define T2 40
#define NT2 3
#define CH 640
#define CC 16
#define LCAP 64
#define NLOG2E (-1.44269504088896340736f)
#define NLN2 (-0.69314718055994530942f)

__device__ __forceinline__ float bf2f(u16 v) { return __uint_as_float(((unsigned)v) << 16); }
__device__ __forceinline__ u16 f2bf(float f) {
    unsigned u = __float_as_uint(f);
    return (u16)((u + 0x7FFFu + ((u >> 16) & 1u)) >> 16);
}
__device__ __forceinline__ float rbf(float f) { return bf2f(f2bf(f)); }
__device__ __forceinline__ void lds_store16(void* p, uint4 v) {
    ((uint2*)p)[0] = make_uint2(v.x, v.y);
    asm volatile("" ::: "memory");
    ((uint2*)p)[1] = make_uint2(v.z, v.w);
}
__device__ __forceinline__ float silu_f(float x) {
    return x * __builtin_amdgcn_rcpf(1.0f + __builtin_amdgcn_exp2f(x * NLOG2E));
}

#define K1_NODES 128
#define K1_LD (D + 8)
__global__ __launch_bounds__(512) void k1_kernel(const u16* __restrict__ h, const u16* __restrict__ w_e,
                                                 const u16* __restrict__ w_h, const int* __restrict__ src,
                                                 const int* __restrict__ dst, const u16* __restrict__ e_attr,
                                                 int* __restrict__ ccount, int2* __restrict__ cells,
                                                 int* __restrict__ ovfcount, int* __restrict__ ovf,
                                                 u16* __restrict__ Pt, u16* __restrict__ Qt, u16* __restrict__ Rt,
                                                 u16* __restrict__ wf16, int n_nodes, int n_edges, int nr, int nch,
                                                 int gemm_tiles) {
    extern __shared__ __attribute__((aligned(16))) char smem[];
    u16* sh = (u16*)smem;
    int* lc = (int*)(smem + 256 * K1_LD * 2);
    int lane = threadIdx.x & 63, wid = __builtin_amdgcn_readfirstlane(threadIdx.x >> 6);
#define TS(i)
    TS(0);
    for (int b = blockIdx.x; b < nch; b += gridDim.x) {
        for (int i = threadIdx.x; i <= nr; i += 512) lc[i] = 0;
        __syncthreads();
        for (int e = b * CH + threadIdx.x; e < min(n_edges, (b + 1) * CH); e += 512) {
            int d = dst[e];
            int r = d / T2;
            int rank = atomicAdd(&lc[r], 1);
            if (rank < CC) {
                cells[((size_t)r * nch + b) * CC + rank] = make_int2(src[e], (int)e_attr[e] | ((d - r * T2) << 16));
            } else {
                int o = atomicAdd(&lc[nr], 1);
                ovf[(size_t)b * CH + o] = e;
            }
        }
        __syncthreads();
        for (int i = threadIdx.x; i < nr; i += 512) ccount[(size_t)i * nch + b] = min(lc[i], CC);
        if (threadIdx.x == 0) ovfcount[b] = lc[nr];
    }
    TS(1);
    for (int b = blockIdx.x; b < gemm_tiles + 16; b += gridDim.x) {
        if (b >= gemm_tiles) {
            int tid = (b - gemm_tiles) * 512 + threadIdx.x;
            int ln = tid & 63, ks = (tid >> 6) & 7, nt = tid >> 9;
            const u16* W = w_h + (size_t)256 * D;
            int n = nt * 16 + (ln & 15), k0 = ks * 32 + 8 * (ln >> 4);
            bf16x8 v;
#pragma unroll
            for (int j = 0; j < 8; ++j) v[j] = (short)W[(size_t)(k0 + j) * D + n];
            *(bf16x8*)(wf16 + (size_t)tid * 8) = v;
            continue;
        }
        int which = b % 3, grp = b / 3;
        int node0 = grp * K1_NODES;
        uint4 v[8];
#pragma unroll
        for (int it = 0; it < 8; ++it) {
            int idx = it * 512 + threadIdx.x;
            int row = idx >> 5, ch = idx & 31;
            int node = min(node0 + row, n_nodes - 1);
            v[it] = *(const uint4*)(h + (size_t)node * D + ch * 8);
        }
        const u16* W = which == 0 ? w_e : (which == 1 ? w_e + (size_t)256 * D : w_h);
        __syncthreads();
        {
            uint4 wv[16];
#pragma unroll
            for (int it = 0; it < 16; ++it) {
                int idx = ((it + grp) & 15) * 512 + threadIdx.x;
                wv[it] = *(const uint4*)(W + (size_t)(idx >> 5) * D + (idx & 31) * 8);
            }
#pragma unroll
            for (int it = 0; it < 16; ++it) {
                int idx = ((it + grp) & 15) * 512 + threadIdx.x;
                lds_store16(sh + (idx >> 5) * K1_LD + (idx & 31) * 8, wv[it]);
            }
        }
        __syncthreads();
        TS(7);
        bf16x8 wa[16];
        {
            int g = lane >> 4, li = lane & 15;
            int krow = 8 * (g >> 1) + (li >> 2);
            int col = wid * 32 + 16 * (g & 1) + 4 * (li & 3);
#pragma unroll
            for (int ks = 0; ks < 16; ++ks) {
                typedef __attribute__((ext_vector_type(4))) short s4;
                typedef __attribute__((address_space(3))) s4* lds_s4p;
                s4 lo = __builtin_amdgcn_ds_read_tr16_b64_v4i16((lds_s4p)(sh + (ks * 16 + krow) * K1_LD + col));
                s4 hi = __builtin_amdgcn_ds_read_tr16_b64_v4i16((lds_s4p)(sh + (ks * 16 + krow + 4) * K1_LD + col));
                wa[ks][0] = lo[0]; wa[ks][1] = lo[1]; wa[ks][2] = lo[2]; wa[ks][3] = lo[3];
                wa[ks][4] = hi[0]; wa[ks][5] = hi[1]; wa[ks][6] = hi[2]; wa[ks][7] = hi[3];
            }
        }
        __syncthreads();
#pragma unroll
        for (int it = 0; it < 8; ++it) {
            int idx = it * 512 + threadIdx.x;
            int row = idx >> 5, ch = idx & 31;
            lds_store16(sh + row * K1_LD + ch * 8, v[it]);
        }
        __syncthreads();
        TS(2);
        const float scale = which < 2 ? NLOG2E : 1.0f;
        u16* T = which == 0 ? Pt : (which == 1 ? Qt : Rt);
#pragma unroll
        for (int tl = 0; tl < K1_NODES / 32; ++tl) {
            f32x16 acc;
#pragma unroll
            for (int r = 0; r < 16; ++r) acc[r] = 0.f;
#pragma unroll
            for (int ks = 0; ks < 16; ++ks) {
                bf16x8 hb = *(const bf16x8*)(sh + (tl * 32 + (lane & 31)) * K1_LD + ks * 16 + 8 * (lane >> 5));
                acc = __builtin_amdgcn_mfma_f32_32x32x16_bf16(wa[ks], hb, acc, 0, 0, 0);
            }
            int mynode = node0 + tl * 32 + (lane & 31);
#pragma unroll
            for (int g = 0; g < 4; ++g) {
                int n = wid * 32 + 8 * g + 4 * (lane >> 5);
                uint2 o;
                o.x = (unsigned)f2bf(acc[4 * g + 0] * scale) | ((unsigned)f2bf(acc[4 * g + 1] * scale) << 16);
                o.y = (unsigned)f2bf(acc[4 * g + 2] * scale) | ((unsigned)f2bf(acc[4 * g + 3] * scale) << 16);
                *(uint2*)(sh + (128 + tl * 32 + (lane & 31)) * K1_LD + n) = o;
            }
        }
        __syncthreads();
#pragma unroll
        for (int it = 0; it < 8; ++it) {
            int idx = it * 512 + threadIdx.x;
            int row = idx >> 5, ch = idx & 31;
            if (node0 + row < n_nodes)
                *(uint4*)(T + (size_t)(node0 + row) * D + ch * 8) = *(const uint4*)(sh + (128 + row) * K1_LD + ch * 8);
        }
        TS(3);
    }
}

#define K2_WAVES 16

struct NodeAcc {
    f2 acc[4];
    float dp0, dp1, dp2;
};

__device__ __forceinline__ void process_chunk(NodeAcc& A, bool valid, int s, float ea, int n, float p0, float p1,
                                              float p2, const f2* P, const f2* wd, const f2* we, const f2* wx,
                                              const u16* __restrict__ pos, const u16* __restrict__ Qt, int lane) {
    int half = lane >> 5, c8 = (lane & 31) * 8;
    unsigned pk_a = 0, pk_b = 0, pk_c = 0;
    if (valid) {
        float d0 = rbf(p0 - bf2f(pos[s * 3 + 0]));
        float d1 = rbf(p1 - bf2f(pos[s * 3 + 1]));
        float d2 = rbf(p2 - bf2f(pos[s * 3 + 2]));
        float dist2 = rbf(rbf(rbf(d0 * d0) + rbf(d1 * d1)) + rbf(d2 * d2));
        pk_a = (__float_as_uint(dist2) >> 16) | (__float_as_uint(ea) & 0xffff0000u);
        pk_b = (__float_as_uint(d0) >> 16) | (__float_as_uint(d1) & 0xffff0000u);
        pk_c = (__float_as_uint(d2) & 0xffff0000u) | 0x3f80u;
    }
    for (int pb = 0; pb * 2 < n; pb += 8) {
        uint4 q[8];
#pragma unroll
        for (int u = 0; u < 8; ++u) {
            int p = pb + u;
            if (2 * p < n) {
                int se = __shfl(s, half * 32 + p);
                q[u] = *(const uint4*)(Qt + (size_t)se * D + c8);
            }
        }
#pragma unroll
        for (int u = 0; u < 8; ++u) {
            int p = pb + u;
            if (2 * p < n) {
                int sl = half * 32 + p;
                unsigned a = __shfl(pk_a, sl), b = __shfl(pk_b, sl), c = __shfl(pk_c, sl);
                float ds = __uint_as_float(a << 16), eae = __uint_as_float(a & 0xffff0000u);
                float vf = __uint_as_float(c << 16);
                f2 ds2 = {ds, ds}, ea2 = {eae, eae}, vf2 = {vf, vf};
                f2 part2 = {0.f, 0.f};
                const unsigned* qw = (const unsigned*)&q[u];
#pragma unroll
                for (int j = 0; j < 4; ++j) {
                    f2 qq = {__uint_as_float(qw[j] << 16), __uint_as_float(qw[j] & 0xffff0000u)};
                    f2 y = (ds2 * wd[j] + (ea2 * we[j] + P[j])) + qq;
                    f2 r = {__builtin_amdgcn_rcpf(1.0f + __builtin_amdgcn_exp2f(y.x)),
                            __builtin_amdgcn_rcpf(1.0f + __builtin_amdgcn_exp2f(y.y))};
                    f2 m = y * r;
                    A.acc[j] = m * vf2 + A.acc[j];
                    part2 = m * wx[j] + part2;
                }
                float part = (part2.x + part2.y) * vf;
                A.dp0 = fmaf(__uint_as_float(b << 16), part, A.dp0);
                A.dp1 = fmaf(__uint_as_float(b & 0xffff0000u), part, A.dp1);
                A.dp2 = fmaf(__uint_as_float(c & 0xffff0000u), part, A.dp2);
            }
        }
    }
}

__global__ __launch_bounds__(1024) void k2_kernel(
    const u16* __restrict__ h, const u16* __restrict__ pos, const int* __restrict__ src, const int* __restrict__ dst,
    const u16* __restrict__ e_attr, const u16* __restrict__ w_e, const u16* __restrict__ w_x,
    const u16* __restrict__ wf16, const int* __restrict__ ccount, const int2* __restrict__ cells,
    const int* __restrict__ ovfcount, const u16* __restrict__ Pt, const u16* __restrict__ Qt,
    const u16* __restrict__ Rt, u16* __restrict__ out_h, u16* __restrict__ out_pos, int n_nodes, int n_edges,
    int nch) {
    __shared__ __attribute__((aligned(16))) u16 sm[NT2 * 16][D + 8];
    __shared__ int2 lst[T2][LCAP];
    __shared__ int ncnt[T2 + 1];
    int lane = threadIdx.x & 63, wid = __builtin_amdgcn_readfirstlane(threadIdx.x >> 6);
    int half = lane >> 5, c8 = (lane & 31) * 8;
#define TS2(i)
    TS2(0);
    int r = blockIdx.x;
    int node0 = r * T2;
    if (threadIdx.x <= T2) ncnt[threadIdx.x] = 0;
    __syncthreads();
    for (int c = threadIdx.x; c < nch; c += 1024) {
        int n = ccount[(size_t)r * nch + c];
        if (ovfcount[c] > 0) ncnt[T2] = 1;
        const int2* cell = cells + ((size_t)r * nch + c) * CC;
        for (int k = 0; k < n; ++k) {
            int2 en = cell[k];
            int dl = en.y >> 16;
            int rank = atomicAdd(&ncnt[dl], 1);
            if (rank < LCAP) lst[dl][rank] = make_int2(en.x, en.y & 0xffff);
        }
    }
    f2 wd[4], we[4], wx[4];
    {
        bf16x8 a = *(const bf16x8*)(w_e + (size_t)512 * D + c8);
        bf16x8 b = *(const bf16x8*)(w_e + (size_t)513 * D + c8);
        bf16x8 c = *(const bf16x8*)(w_x + c8);
#pragma unroll
        for (int j = 0; j < 4; ++j) {
            wd[j] = (f2){bf2f((u16)a[2 * j]) * NLOG2E, bf2f((u16)a[2 * j + 1]) * NLOG2E};
            we[j] = (f2){bf2f((u16)b[2 * j]) * NLOG2E, bf2f((u16)b[2 * j + 1]) * NLOG2E};
            wx[j] = (f2){bf2f((u16)c[2 * j]), bf2f((u16)c[2 * j + 1])};
        }
    }
    __syncthreads();
    TS2(1);
    int kent = 2 * (lane & 31) + half;
    for (int t = wid; t < T2; t += K2_WAVES) {
        int i = node0 + t;
        NodeAcc A;
#pragma unroll
        for (int j = 0; j < 4; ++j) A.acc[j] = (f2){0.f, 0.f};
        A.dp0 = A.dp1 = A.dp2 = 0.f;
        if (i < n_nodes) {
            int c = __builtin_amdgcn_readfirstlane(ncnt[t]);
            bool slow = (c > LCAP) || (__builtin_amdgcn_readfirstlane(ncnt[T2]) != 0);
            float p0 = bf2f(pos[i * 3 + 0]), p1 = bf2f(pos[i * 3 + 1]), p2 = bf2f(pos[i * 3 + 2]);
            f2 P[4];
            {
                uint4 a = *(const uint4*)(Pt + (size_t)i * D + c8);
                const unsigned* w = (const unsigned*)&a;
#pragma unroll
                for (int j = 0; j < 4; ++j)
                    P[j] = (f2){__uint_as_float(w[j] << 16), __uint_as_float(w[j] & 0xffff0000u)};
            }
            if (!slow) {
                bool valid = kent < c;
                int2 b = valid ? lst[t][kent] : make_int2(0, 0);
                process_chunk(A, valid, b.x, bf2f((u16)b.y), c, p0, p1, p2, P, wd, we, wx, pos, Qt, lane);
            } else {
                for (int e0 = 0; e0 < n_edges; e0 += 64) {
                    int e = e0 + kent;
                    bool v2 = e < n_edges && dst[e] == i;
                    if (__ballot(v2)) {
                        int s2 = v2 ? src[e] : 0;
                        float ea2 = v2 ? bf2f(e_attr[e]) : 0.f;
                        process_chunk(A, v2, s2, ea2, 64, p0, p1, p2, P, wd, we, wx, pos, Qt, lane);
                    }
                }
            }
#pragma unroll
            for (int j = 0; j < 4; ++j) {
                A.acc[j].x += __shfl_xor(A.acc[j].x, 32);
                A.acc[j].y += __shfl_xor(A.acc[j].y, 32);
            }
#pragma unroll
            for (int o = 32; o > 0; o >>= 1) {
                A.dp0 += __shfl_xor(A.dp0, o);
                A.dp1 += __shfl_xor(A.dp1, o);
                A.dp2 += __shfl_xor(A.dp2, o);
            }
            if (lane == 0) {
                out_pos[i * 3 + 0] = f2bf(p0 + A.dp0 * NLN2);
                out_pos[i * 3 + 1] = f2bf(p1 + A.dp1 * NLN2);
                out_pos[i * 3 + 2] = f2bf(p2 + A.dp2 * NLN2);
            }
        }
        if (half == 0) {
            bf16x8 o;
#pragma unroll
            for (int j = 0; j < 4; ++j) {
                o[2 * j] = (short)f2bf(A.acc[j].x * NLN2);
                o[2 * j + 1] = (short)f2bf(A.acc[j].y * NLN2);
            }
            lds_store16(&sm[t][c8], *(uint4*)&o);
        }
    }
    TS2(2);
    bf16x8 wa[8];
#pragma unroll
    for (int ks = 0; ks < 8; ++ks) wa[ks] = *(const bf16x8*)(wf16 + ((size_t)(wid * 8 + ks) * 64 + lane) * 8);
    __syncthreads();
    TS2(3);
    f32x4 acc2[NT2];
#pragma unroll
    for (int t = 0; t < NT2; ++t)
#pragma unroll
        for (int rr = 0; rr < 4; ++rr) acc2[t][rr] = 0.f;
#pragma unroll
    for (int ks = 0; ks < 8; ++ks) {
#pragma unroll
        for (int t = 0; t < NT2; ++t) {
            bf16x8 mb = *(const bf16x8*)(&sm[t * 16 + (lane & 15)][ks * 32 + 8 * (lane >> 4)]);
            acc2[t] = __builtin_amdgcn_mfma_f32_16x16x32_bf16(wa[ks], mb, acc2[t], 0, 0, 0);
        }
    }
#pragma unroll
    for (int t = 0; t < NT2; ++t) {
        int mynode = node0 + t * 16 + (lane & 15);
        if (t * 16 + (lane & 15) < T2 && mynode < n_nodes) {
            int n = wid * 16 + 4 * (lane >> 4);
            ushort4 rr = *(const ushort4*)(Rt + (size_t)mynode * D + n);
            ushort4 hh = *(const ushort4*)(h + (size_t)mynode * D + n);
            ushort4 o;
            o.x = f2bf(bf2f(hh.x) + silu_f(acc2[t][0] + bf2f(rr.x)));
            o.y = f2bf(bf2f(hh.y) + silu_f(acc2[t][1] + bf2f(rr.y)));
            o.z = f2bf(bf2f(hh.z) + silu_f(acc2[t][2] + bf2f(rr.z)));
            o.w = f2bf(bf2f(hh.w) + silu_f(acc2[t][3] + bf2f(rr.w)));
            *(ushort4*)(out_h + (size_t)mynode * D + n) = o;
        }
    }
}

extern "C" void kernel_launch(void* const* d_in, const int* in_sizes, int n_in, void* d_out, int out_size,
                              void* d_ws, size_t ws_size, hipStream_t stream) {
    const u16* h = (const u16*)d_in[0];
    const u16* pos = (const u16*)d_in[1];
    const u16* e_attr = (const u16*)d_in[2];
    const int* src = (const int*)d_in[3];
    const int* dst = (const int*)d_in[4];
    const u16* w_e = (const u16*)d_in[5];
    const u16* w_x = (const u16*)d_in[6];
    const u16* w_h = (const u16*)d_in[7];
    int n_nodes = in_sizes[0] / D;
    int n_edges = in_sizes[3];
    u16* out_h = (u16*)d_out;
    u16* out_pos = out_h + (size_t)n_nodes * D;
    int nr = (n_nodes + T2 - 1) / T2;
    int nch = (n_edges + CH - 1) / CH;

    char* ws = (char*)d_ws;
    size_t off = 0;
    auto take = [&](size_t bytes) { char* p = ws + off; off += (bytes + 255) & ~(size_t)255; return p; };
    int* ccount = (int*)take((size_t)nr * nch * 4);
    int2* cells = (int2*)take((size_t)nr * nch * CC * 8);
    int* ovfcount = (int*)take((size_t)nch * 4);
    int* ovf = (int*)take((size_t)nch * CH * 4);
    u16* Pt = (u16*)take((size_t)n_nodes * D * 2);
    u16* Qt = (u16*)take((size_t)n_nodes * D * 2);
    u16* Rt = (u16*)take((size_t)n_nodes * D * 2);
    u16* wf16 = (u16*)take((size_t)16 * 8 * 64 * 8 * 2);

    int gemm_tiles = 3 * ((n_nodes + K1_NODES - 1) / K1_NODES);
    size_t lds1 = (size_t)256 * K1_LD * 2 + (size_t)(nr + 1) * 4;
    int grid1 = max(nch, gemm_tiles + 16);
    if (grid1 > 256) grid1 = 256;
    k1_kernel<<<grid1, 512, lds1, stream>>>(h, w_e, w_h, src, dst, e_attr, ccount, cells, ovfcount, ovf, Pt, Qt, Rt,
                                            wf16, n_nodes, n_edges, nr, nch, gemm_tiles);
    k2_kernel<<<nr, 1024, 0, stream>>>(h, pos, src, dst, e_attr, w_e, w_x, wf16, ccount, cells, ovfcount, Pt, Qt, Rt,
                                       out_h, out_pos, n_nodes, n_edges, nch);
}
```

```cpp
#include <hip/hip_runtime.h>
#include <hip/hip_bf16.h>
#include <stdint.h>


typedef unsigned short u16;
using bf16x8 = __attribute__((ext_vector_type(8))) short;
using f32x16 = __attribute__((ext_vector_type(16))) float;
using f32x4 = __attribute__((ext_vector_type(4))) float;
typedef float f2 __attribute__((ext_vector_type(2)));

#define D 256
#define T2 40
#define NT2 3
#define CH 640
#define CC 16
#define LCAP 64
#define NLOG2E (-1.44269504088896340736f)
#define NLN2 (-0.69314718055994530942f)

__device__ __forceinline__ float bf2f(u16 v) { return __uint_as_float(((unsigned)v) << 16); }
__device__ __forceinline__ u16 f2bf(float f) {
    unsigned u = __float_as_uint(f);
    return (u16)((u + 0x7FFFu + ((u >> 16) & 1u)) >> 16);
}
__device__ __forceinline__ float rbf(float f) { return bf2f(f2bf(f)); }
__device__ __forceinline__ void lds_store16(void* p, uint4 v) {
    ((uint2*)p)[0] = make_uint2(v.x, v.y);
    asm volatile("" ::: "memory");
    ((uint2*)p)[1] = make_uint2(v.z, v.w);
}
__device__ __forceinline__ float silu_f(float x) {
    return x * __builtin_amdgcn_rcpf(1.0f + __builtin_amdgcn_exp2f(x * NLOG2E));
}

#define K1_NODES 128
#define K1_LD (D + 8)
__global__ __launch_bounds__(512) void k1_kernel(const u16* __restrict__ h, const u16* __restrict__ w_e,
                                                 const u16* __restrict__ w_h, const int* __restrict__ src,
                                                 const int* __restrict__ dst, const u16* __restrict__ e_attr,
                                                 int* __restrict__ ccount, int2* __restrict__ cells,
                                                 int* __restrict__ ovfcount, int* __restrict__ ovf,
                                                 u16* __restrict__ Pt, u16* __restrict__ Qt, u16* __restrict__ Rt,
                                                 u16* __restrict__ wf16, int n_nodes, int n_edges, int nr, int nch,
                                                 int gemm_tiles) {
    extern __shared__ __attribute__((aligned(16))) char smem[];
    u16* sh = (u16*)smem;
    int* lc = (int*)(smem + 256 * K1_LD * 2);
    int lane = threadIdx.x & 63, wid = __builtin_amdgcn_readfirstlane(threadIdx.x >> 6);
    uint4 v[8], wv[16];
    bool pre = (int)blockIdx.x < gemm_tiles;
    if (pre) {
        int which = blockIdx.x % 3, grp = blockIdx.x / 3;
        const u16* W = which == 0 ? w_e : (which == 1 ? w_e + (size_t)256 * D : w_h);
#pragma unroll
        for (int it = 0; it < 8; ++it) {
            int idx = it * 512 + threadIdx.x;
            int node = min(grp * K1_NODES + (idx >> 5), n_nodes - 1);
            v[it] = *(const uint4*)(h + (size_t)node * D + (idx & 31) * 8);
        }
#pragma unroll
        for (int it = 0; it < 16; ++it) {
            int idx = ((it + grp) & 15) * 512 + threadIdx.x;
            wv[it] = *(const uint4*)(W + (size_t)(idx >> 5) * D + (idx & 31) * 8);
        }
    }
    for (int b = blockIdx.x; b < nch; b += gridDim.x) {
        for (int i = threadIdx.x; i <= nr; i += 512) lc[i] = 0;
        __syncthreads();
        for (int e = b * CH + threadIdx.x; e < min(n_edges, (b + 1) * CH); e += 512) {
            int d = dst[e];
            int r = d / T2;
            int rank = atomicAdd(&lc[r], 1);
            if (rank < CC) {
                cells[((size_t)r * nch + b) * CC + rank] = make_int2(src[e], (int)e_attr[e] | ((d - r * T2) << 16));
            } else {
                int o = atomicAdd(&lc[nr], 1);
                ovf[(size_t)b * CH + o] = e;
            }
        }
        __syncthreads();
        for (int i = threadIdx.x; i < nr; i += 512) ccount[(size_t)i * nch + b] = min(lc[i], CC);
        if (threadIdx.x == 0) ovfcount[b] = lc[nr];
    }
    for (int b = blockIdx.x; b < gemm_tiles + 16; b += gridDim.x) {
        if (b >= gemm_tiles) {
            int tid = (b - gemm_tiles) * 512 + threadIdx.x;
            int ln = tid & 63, ks = (tid >> 6) & 7, nt = tid >> 9;
            const u16* W = w_h + (size_t)256 * D;
            int n = nt * 16 + (ln & 15), k0 = ks * 32 + 8 * (ln >> 4);
            bf16x8 vv;
#pragma unroll
            for (int j = 0; j < 8; ++j) vv[j] = (short)W[(size_t)(k0 + j) * D + n];
            *(bf16x8*)(wf16 + (size_t)tid * 8) = vv;
            continue;
        }
        int which = b % 3, grp = b / 3;
        int node0 = grp * K1_NODES;
        if (b != (int)blockIdx.x) {
            const u16* W = which == 0 ? w_e : (which == 1 ? w_e + (size_t)256 * D : w_h);
#pragma unroll
            for (int it = 0; it < 8; ++it) {
                int idx = it * 512 + threadIdx.x;
                int node = min(node0 + (idx >> 5), n_nodes - 1);
                v[it] = *(const uint4*)(h + (size_t)node * D + (idx & 31) * 8);
            }
#pragma unroll
            for (int it = 0; it < 16; ++it) {
                int idx = ((it + grp) & 15) * 512 + threadIdx.x;
                wv[it] = *(const uint4*)(W + (size_t)(idx >> 5) * D + (idx & 31) * 8);
            }
        }
        __syncthreads();
#pragma unroll
        for (int it = 0; it < 16; ++it) {
            int idx = ((it + grp) & 15) * 512 + threadIdx.x;
            lds_store16(sh + (idx >> 5) * K1_LD + (idx & 31) * 8, wv[it]);
        }
        __syncthreads();
        bf16x8 wa[16];
        {
            int g = lane >> 4, li = lane & 15;
            int krow = 8 * (g >> 1) + (li >> 2);
            int col = wid * 32 + 16 * (g & 1) + 4 * (li & 3);
#pragma unroll
            for (int ks = 0; ks < 16; ++ks) {
                typedef __attribute__((ext_vector_type(4))) short s4;
                typedef __attribute__((address_space(3))) s4* lds_s4p;
                s4 lo = __builtin_amdgcn_ds_read_tr16_b64_v4i16((lds_s4p)(sh + (ks * 16 + krow) * K1_LD + col));
                s4 hi = __builtin_amdgcn_ds_read_tr16_b64_v4i16((lds_s4p)(sh + (ks * 16 + krow + 4) * K1_LD + col));
                wa[ks][0] = lo[0]; wa[ks][1] = lo[1]; wa[ks][2] = lo[2]; wa[ks][3] = lo[3];
                wa[ks][4] = hi[0]; wa[ks][5] = hi[1]; wa[ks][6] = hi[2]; wa[ks][7] = hi[3];
            }
        }
        __syncthreads();
#pragma unroll
        for (int it = 0; it < 8; ++it) {
            int idx = it * 512 + threadIdx.x;
            int row = idx >> 5, ch = idx & 31;
            lds_store16(sh + row * K1_LD + ch * 8, v[it]);
        }
        __syncthreads();
        const float scale = which < 2 ? NLOG2E : 1.0f;
        u16* T = which == 0 ? Pt : (which == 1 ? Qt : Rt);
#pragma unroll
        for (int tl = 0; tl < K1_NODES / 32; ++tl) {
            f32x16 acc;
#pragma unroll
            for (int r = 0; r < 16; ++r) acc[r] = 0.f;
#pragma unroll
            for (int ks = 0; ks < 16; ++ks) {
                bf16x8 hb = *(const bf16x8*)(sh + (tl * 32 + (lane & 31)) * K1_LD + ks * 16 + 8 * (lane >> 5));
                acc = __builtin_amdgcn_mfma_f32_32x32x16_bf16(wa[ks], hb, acc, 0, 0, 0);
            }
#pragma unroll
            for (int g = 0; g < 4; ++g) {
                int n = wid * 32 + 8 * g + 4 * (lane >> 5);
                uint2 o;
                o.x = (unsigned)f2bf(acc[4 * g + 0] * scale) | ((unsigned)f2bf(acc[4 * g + 1] * scale) << 16);
                o.y = (unsigned)f2bf(acc[4 * g + 2] * scale) | ((unsigned)f2bf(acc[4 * g + 3] * scale) << 16);
                *(uint2*)(sh + (128 + tl * 32 + (lane & 31)) * K1_LD + n) = o;
            }
        }
        __syncthreads();
#pragma unroll
        for (int it = 0; it < 8; ++it) {
            int idx = it * 512 + threadIdx.x;
            int row = idx >> 5, ch = idx & 31;
            if (node0 + row < n_nodes)
                *(uint4*)(T + (size_t)(node0 + row) * D + ch * 8) = *(const uint4*)(sh + (128 + row) * K1_LD + ch * 8);
        }
    }
}

#define K2_WAVES 16

struct NodeAcc {
    f2 acc[4];
    float dp0, dp1, dp2;
};

__device__ __forceinline__ void process_chunk(NodeAcc& A, bool valid, int s, float ea, int n, float p0, float p1,
                                              float p2, const f2* P, const f2* wd, const f2* we, const f2* wx,
                                              const u16* __restrict__ pos, const u16* __restrict__ Qt, int lane) {
    int half = lane >> 5, c8 = (lane & 31) * 8;
    unsigned pk_a = 0, pk_b = 0, pk_c = 0;
    if (valid) {
        float d0 = rbf(p0 - bf2f(pos[s * 3 + 0]));
        float d1 = rbf(p1 - bf2f(pos[s * 3 + 1]));
        float d2 = rbf(p2 - bf2f(pos[s * 3 + 2]));
        float dist2 = rbf(rbf(rbf(d0 * d0) + rbf(d1 * d1)) + rbf(d2 * d2));
        pk_a = (__float_as_uint(dist2) >> 16) | (__float_as_uint(ea) & 0xffff0000u);
        pk_b = (__float_as_uint(d0) >> 16) | (__float_as_uint(d1) & 0xffff0000u);
        pk_c = (__float_as_uint(d2) & 0xffff0000u) | 0x3f80u;
    }
    for (int pb = 0; pb * 2 < n; pb += 8) {
        uint4 q[8];
#pragma unroll
        for (int u = 0; u < 8; ++u) {
            int p = pb + u;
            if (2 * p < n) {
                int se = __shfl(s, half * 32 + p);
                q[u] = *(const uint4*)(Qt + (size_t)se * D + c8);
            }
        }
#pragma unroll
        for (int u = 0; u < 8; ++u) {
            int p = pb + u;
            if (2 * p < n) {
                int sl = half * 32 + p;
                unsigned a = __shfl(pk_a, sl), b = __shfl(pk_b, sl), c = __shfl(pk_c, sl);
                float ds = __uint_as_float(a << 16), eae = __uint_as_float(a & 0xffff0000u);
                float vf = __uint_as_float(c << 16);
                f2 ds2 = {ds, ds}, ea2 = {eae, eae}, vf2 = {vf, vf};
                f2 part2 = {0.f, 0.f};
                const unsigned* qw = (const unsigned*)&q[u];
#pragma unroll
                for (int j = 0; j < 4; ++j) {
                    f2 qq = {__uint_as_float(qw[j] << 16), __uint_as_float(qw[j] & 0xffff0000u)};
                    f2 y = (ds2 * wd[j] + (ea2 * we[j] + P[j])) + qq;
                    f2 r = {__builtin_amdgcn_rcpf(1.0f + __builtin_amdgcn_exp2f(y.x)),
                            __builtin_amdgcn_rcpf(1.0f + __builtin_amdgcn_exp2f(y.y))};
                    f2 m = y * r;
                    A.acc[j] = m * vf2 + A.acc[j];
                    part2 = m * wx[j] + part2;
                }
                float part = (part2.x + part2.y) * vf;
                A.dp0 = fmaf(__uint_as_float(b << 16), part, A.dp0);
                A.dp1 = fmaf(__uint_as_float(b & 0xffff0000u), part, A.dp1);
                A.dp2 = fmaf(__uint_as_float(c & 0xffff0000u), part, A.dp2);
            }
        }
    }
}

__global__ __launch_bounds__(1024) void k2_kernel(
    const u16* __restrict__ h, const u16* __restrict__ pos, const int* __restrict__ src, const int* __restrict__ dst,
    const u16* __restrict__ e_attr, const u16* __restrict__ w_e, const u16* __restrict__ w_x,
    const u16* __restrict__ wf16, const int* __restrict__ ccount, const int2* __restrict__ cells,
    const int* __restrict__ ovfcount, const u16* __restrict__ Pt, const u16* __restrict__ Qt,
    const u16* __restrict__ Rt, u16* __restrict__ out_h, u16* __restrict__ out_pos, int n_nodes, int n_edges,
    int nch) {
    __shared__ __attribute__((aligned(16))) u16 sm[NT2 * 16][D + 8];
    __shared__ int2 lst[T2][LCAP];
    __shared__ int ncnt[T2 + 2];
    int lane = threadIdx.x & 63, wid = __builtin_amdgcn_readfirstlane(threadIdx.x >> 6);
    int half = lane >> 5, c8 = (lane & 31) * 8;
    int r = blockIdx.x;
    int node0 = r * T2;
    if (threadIdx.x <= T2 + 1) ncnt[threadIdx.x] = threadIdx.x == T2 + 1 ? K2_WAVES : 0;
    __syncthreads();
    for (int c = threadIdx.x; c < nch; c += 1024) {
        const int2* cell = cells + ((size_t)r * nch + c) * CC;
        int n = ccount[(size_t)r * nch + c];
        int oc = ovfcount[c];
        int4 f0 = *(const int4*)cell, f1 = *(const int4*)(cell + 2);
        if (oc > 0) ncnt[T2] = 1;
        for (int k = 0; k < n; ++k) {
            int2 en = k == 0 ? make_int2(f0.x, f0.y) : k == 1 ? make_int2(f0.z, f0.w) : k == 2 ? make_int2(f1.x, f1.y)
                    : k == 3 ? make_int2(f1.z, f1.w) : cell[k];
            int dl = en.y >> 16;
            int rank = atomicAdd(&ncnt[dl], 1);
            if (rank < LCAP) lst[dl][rank] = make_int2(en.x, en.y & 0xffff);
        }
    }
    f2 wd[4], we[4], wx[4];
    {
        bf16x8 a = *(const bf16x8*)(w_e + (size_t)512 * D + c8);
        bf16x8 b = *(const bf16x8*)(w_e + (size_t)513 * D + c8);
        bf16x8 c = *(const bf16x8*)(w_x + c8);
#pragma unroll
        for (int j = 0; j < 4; ++j) {
            wd[j] = (f2){bf2f((u16)a[2 * j]) * NLOG2E, bf2f((u16)a[2 * j + 1]) * NLOG2E};
            we[j] = (f2){bf2f((u16)b[2 * j]) * NLOG2E, bf2f((u16)b[2 * j + 1]) * NLOG2E};
            wx[j] = (f2){bf2f((u16)c[2 * j]), bf2f((u16)c[2 * j + 1])};
        }
    }
    __syncthreads();
    int kent = 2 * (lane & 31) + half;
    for (int t = wid; t < T2;) {
        int i = node0 + t;
        NodeAcc A;
#pragma unroll
        for (int j = 0; j < 4; ++j) A.acc[j] = (f2){0.f, 0.f};
        A.dp0 = A.dp1 = A.dp2 = 0.f;
        if (i < n_nodes) {
            int c = __builtin_amdgcn_readfirstlane(ncnt[t]);
            bool slow = (c > LCAP) || (__builtin_amdgcn_readfirstlane(ncnt[T2]) != 0);
            float p0 = bf2f(pos[i * 3 + 0]), p1 = bf2f(pos[i * 3 + 1]), p2 = bf2f(pos[i * 3 + 2]);
            f2 P[4];
            {
                uint4 a = *(const uint4*)(Pt + (size_t)i * D + c8);
                const unsigned* w = (const unsigned*)&a;
#pragma unroll
                for (int j = 0; j < 4; ++j)
                    P[j] = (f2){__uint_as_float(w[j] << 16), __uint_as_float(w[j] & 0xffff0000u)};
            }
            if (!slow) {
                bool valid = kent < c;
                int2 b = valid ? lst[t][kent] : make_int2(0, 0);
                {
                    int rank = 0;
                    unsigned klo = ((unsigned)b.x << 22) | ((unsigned)b.y << 6) | (unsigned)kent;
                    unsigned khi = (unsigned)b.x >> 10;
                    unsigned long long key = ((unsigned long long)khi << 32) | klo;
                    for (int j = 0; j < c; ++j) {
                        int lj = (j & 1) * 32 + (j >> 1);
                        unsigned lo = __builtin_amdgcn_readlane(klo, lj), hi = __builtin_amdgcn_readlane(khi, lj);
                        rank += ((((unsigned long long)hi << 32) | lo) < key) ? 1 : 0;
                    }
                    if (valid) lst[t][rank] = b;
                    b = valid ? lst[t][kent] : make_int2(0, 0);
                }
                process_chunk(A, valid, b.x, bf2f((u16)b.y), c, p0, p1, p2, P, wd, we, wx, pos, Qt, lane);
            } else {
                for (int e0 = 0; e0 < n_edges; e0 += 64) {
                    int e = e0 + kent;
                    bool v2 = e < n_edges && dst[e] == i;
                    if (__ballot(v2)) {
                        int s2 = v2 ? src[e] : 0;
                        float ea2 = v2 ? bf2f(e_attr[e]) : 0.f;
                        process_chunk(A, v2, s2, ea2, 64, p0, p1, p2, P, wd, we, wx, pos, Qt, lane);
                    }
                }
            }
#pragma unroll
            for (int j = 0; j < 4; ++j) {
                A.acc[j].x += __shfl_xor(A.acc[j].x, 32);
                A.acc[j].y += __shfl_xor(A.acc[j].y, 32);
            }
#pragma unroll
            for (int o = 32; o > 0; o >>= 1) {
                A.dp0 += __shfl_xor(A.dp0, o);
                A.dp1 += __shfl_xor(A.dp1, o);
                A.dp2 += __shfl_xor(A.dp2, o);
            }
            if (lane == 0) {
                out_pos[i * 3 + 0] = f2bf(p0 + A.dp0 * NLN2);
                out_pos[i * 3 + 1] = f2bf(p1 + A.dp1 * NLN2);
                out_pos[i * 3 + 2] = f2bf(p2 + A.dp2 * NLN2);
            }
        }
        if (half == 0) {
            bf16x8 o;
#pragma unroll
            for (int j = 0; j < 4; ++j) {
                o[2 * j] = (short)f2bf(A.acc[j].x * NLN2);
                o[2 * j + 1] = (short)f2bf(A.acc[j].y * NLN2);
            }
            lds_store16(&sm[t][c8], *(uint4*)&o);
        }
        int tn = 0;
        if (lane == 0) tn = atomicAdd(&ncnt[T2 + 1], 1);
        t = __builtin_amdgcn_readfirstlane(tn);
    }
    ushort4 rpre[NT2], hpre[NT2];
#pragma unroll
    for (int t = 0; t < NT2; ++t) {
        int mynode = min(node0 + t * 16 + (lane & 15), n_nodes - 1);
        int n = wid * 16 + 4 * (lane >> 4);
        rpre[t] = *(const ushort4*)(Rt + (size_t)mynode * D + n);
        hpre[t] = *(const ushort4*)(h + (size_t)mynode * D + n);
    }
    bf16x8 wa[8];
#pragma unroll
    for (int ks = 0; ks < 8; ++ks) wa[ks] = *(const bf16x8*)(wf16 + ((size_t)(wid * 8 + ks) * 64 + lane) * 8);
    __syncthreads();
    f32x4 acc2[NT2];
#pragma unroll
    for (int t = 0; t < NT2; ++t)
#pragma unroll
        for (int rr = 0; rr < 4; ++rr) acc2[t][rr] = 0.f;
#pragma unroll
    for (int ks = 0; ks < 8; ++ks) {
#pragma unroll
        for (int t = 0; t < NT2; ++t) {
            bf16x8 mb = *(const bf16x8*)(&sm[t * 16 + (lane & 15)][ks * 32 + 8 * (lane >> 4)]);
            acc2[t] = __builtin_amdgcn_mfma_f32_16x16x32_bf16(wa[ks], mb, acc2[t], 0, 0, 0);
        }
    }
#pragma unroll
    for (int t = 0; t < NT2; ++t) {
        int mynode = node0 + t * 16 + (lane & 15);
        if (t * 16 + (lane & 15) < T2 && mynode < n_nodes) {
            int n = wid * 16 + 4 * (lane >> 4);
            ushort4 rr = rpre[t];
            ushort4 hh = hpre[t];
            ushort4 o;
            o.x = f2bf(bf2f(hh.x) + silu_f(acc2[t][0] + bf2f(rr.x)));
            o.y = f2bf(bf2f(hh.y) + silu_f(acc2[t][1] + bf2f(rr.y)));
            o.z = f2bf(bf2f(hh.z) + silu_f(acc2[t][2] + bf2f(rr.z)));
            o.w = f2bf(bf2f(hh.w) + silu_f(acc2[t][3] + bf2f(rr.w)));
            *(ushort4*)(out_h + (size_t)mynode * D + n) = o;
        }
    }
}

extern "C" void kernel_launch(void* const* d_in, const int* in_sizes, int n_in, void* d_out, int out_size,
                              void* d_ws, size_t ws_size, hipStream_t stream) {
    const u16* h = (const u16*)d_in[0];
    const u16* pos = (const u16*)d_in[1];
    const u16* e_attr = (const u16*)d_in[2];
    const int* src = (const int*)d_in[3];
    const int* dst = (const int*)d_in[4];
    const u16* w_e = (const u16*)d_in[5];
    const u16* w_x = (const u16*)d_in[6];
    const u16* w_h = (const u16*)d_in[7];
    int n_nodes = in_sizes[0] / D;
    int n_edges = in_sizes[3];
    u16* out_h = (u16*)d_out;
    u16* out_pos = out_h + (size_t)n_nodes * D;
    int nr = (n_nodes + T2 - 1) / T2;
    int nch = (n_edges + CH - 1) / CH;

    char* ws = (char*)d_ws;
    size_t off = 0;
    auto take = [&](size_t bytes) { char* p = ws + off; off += (bytes + 255) & ~(size_t)255; return p; };
    int* ccount = (int*)take((size_t)nr * nch * 4);
    int2* cells = (int2*)take((size_t)nr * nch * CC * 8);
    int* ovfcount = (int*)take((size_t)nch * 4);
    int* ovf = (int*)take((size_t)nch * CH * 4);
    u16* Pt = (u16*)take((size_t)n_nodes * D * 2);
    u16* Qt = (u16*)take((size_t)n_nodes * D * 2);
    u16* Rt = (u16*)take((size_t)n_nodes * D * 2);
    u16* wf16 = (u16*)take((size_t)16 * 8 * 64 * 8 * 2);

    int gemm_tiles = 3 * ((n_nodes + K1_NODES - 1) / K1_NODES);
    size_t lds1 = (size_t)256 * K1_LD * 2 + (size_t)(nr + 1) * 4;
    int grid1 = max(nch, gemm_tiles + 16);
    if (grid1 > 256) grid1 = 256;
    k1_kernel<<<grid1, 512, lds1, stream>>>(h, w_e, w_h, src, dst, e_attr, ccount, cells, ovfcount, ovf, Pt, Qt, Rt,
                                            wf16, n_nodes, n_edges, nr, nch, gemm_tiles);
    k2_kernel<<<nr, 1024, 0, stream>>>(h, pos, src, dst, e_attr, w_e, w_x, wf16, ccount, cells, ovfcount, Pt, Qt, Rt,
                                       out_h, out_pos, n_nodes, n_edges, nch);
}
```
